```python
import torch

def _softmax(x, axis=-1):
    return torch.softmax(x, dim=axis)
def _gelu(x):
    return torch.nn.functional.gelu(x, approximate="tanh")
def _silu(x):
    return torch.nn.functional.silu(x)
def _sigmoid(x):
    return torch.sigmoid(x)
def _layernorm(x, axis=-1, eps=1e-5):
    m = x.mean(dim=axis, keepdim=True)
    v = x.var(dim=axis, keepdim=True, unbiased=False)
    return (x - m) / torch.sqrt(v + eps)
def _scatter_add(tgt, idx, src, axis=0):
    return tgt.clone().index_add_(axis, idx, src)
def _segment_sum(src, seg, n):
    out = torch.zeros((n,) + src.shape[1:], dtype=src.dtype)
    return out.index_add_(0, seg, src)
def _pad(x, pw):
    flat = tuple(v for pair in reversed(pw) for v in pair)
    return torch.nn.functional.pad(x, flat)

kernel_name = 'gated_mix__sliding_block__mixer_token_mlp__prompt'

def setup_inputs():

    _w = (lambda a: (lambda t: t.to(torch.bfloat16) if t.is_floating_point() else t)(torch.as_tensor(a)))
    def _A_setup():
        B, S, D = 1, 2048, 512
        return {
            "x": _w((lambda *s: torch.randn(*s, dtype=torch.bfloat16) * 0.05)(B, S, D)),
            "wqkv": _w((lambda *s: torch.randn(*s, dtype=torch.bfloat16) * 0.8)(D, 3 * D)),
            "wo": _w((lambda *s: torch.randn(*s, dtype=torch.bfloat16) * 0.05)(D, D)),
        }
    _A_in = _A_setup()

    def _B_setup():
        B, S, D, F = 1, 2048, 512, 1376
        return {
            "x": _w((lambda *s: torch.randn(*s, dtype=torch.bfloat16) * 0.05)(B, S, D)),
            "w1": _w((lambda *s: torch.randn(*s, dtype=torch.bfloat16) * 3.2)(S, F) * (S ** -0.5)),
            "b1": _w((lambda *s: torch.randn(*s, dtype=torch.bfloat16) * 3.2)(F) * 0.02),
            "w2": _w((lambda *s: torch.randn(*s, dtype=torch.bfloat16) * 0.1)(F, S) * (F ** -0.5)),
            "b2": _w((lambda *s: torch.randn(*s, dtype=torch.bfloat16) * 0.1)(S) * 0.02),
        }
    _B_in = _B_setup()

    return {
        "A_x": _A_in["x"],
        "A_wqkv": _A_in["wqkv"],
        "A_wo": _A_in["wo"],
        "B_x": _B_in["x"],
        "B_w1": _B_in["w1"],
        "B_b1": _B_in["b1"],
        "B_w2": _B_in["w2"],
        "B_b2": _B_in["b2"],
        "gate_w": _w((lambda *s: torch.randn(*s, dtype=torch.bfloat16) * 0.05)(512) * 16.0),
    }

def reference(A_x, A_wqkv, A_wo, B_x, B_w1, B_b1, B_w2, B_b2, gate_w):
    def _A_ref(x, wqkv, wo):
        B, S, D = x.shape
        H = 32; Dh = D // H; W = 256
        qkv = (lambda x, *d: x.permute(*d))((x @ wqkv).reshape(B, S, 3, H, Dh), 2, 0, 3, 1, 4)
        q, k, v = qkv[0], qkv[1], qkv[2]
        scores = torch.einsum("bhqd,bhkd->bhqk", q, k) * (Dh ** -0.5)
        idx = torch.arange(S)
        mask = (idx[:, None] - idx[None, :] >= 0) & (idx[:, None] - idx[None, :] < W)
        scores = torch.where(mask[None, None], scores, -1e9)
        p = _softmax(scores, axis=-1)
        o = torch.einsum("bhqk,bhkd->bhqd", p, v)
        return (lambda x, *d: x.permute(*d))(o, 0, 2, 1, 3).reshape(B, S, D) @ wo

    def _B_ref(x, w1, b1, w2, b2):
        B, S, D = x.shape
        h = _layernorm(x, axis=-1)
        u = torch.einsum("bsd,sf->bdf", h, w1) + b1
        u = _gelu(u)
        y = torch.einsum("bdf,fs->bds", u, w2) + b2
        return (lambda x, *d: x.permute(*d))(y, 0, 2, 1)

    x = A_x
    g = _sigmoid((x * gate_w).sum(-1, keepdims=True))
    a = _A_ref(x, A_wqkv, A_wo)
    b = _B_ref(x, B_w1, B_b1, B_w2, B_b2)
    return g * a + (1 - g) * b
```

```cpp
#include <hip/hip_runtime.h>
#include <hip/hip_bf16.h>
typedef __hip_bfloat16 bf16;
__device__ __forceinline__ float b2f(bf16 v){return __bfloat162float(v);}
constexpr int S=2048,D=512,F=1376,H=32,DH=16,W=256;

__global__ void k_qkv(const bf16* x,const bf16* w,bf16* qkv){
  int n=blockIdx.x*256+threadIdx.x; int m=blockIdx.y;
  float acc=0; for(int k=0;k<D;k++) acc+=b2f(x[m*D+k])*b2f(w[k*1536+n]);
  qkv[m*1536+n]=__float2bfloat16(acc);
}
__global__ void k_ln(const bf16* x,const bf16* gw,bf16* h,float* g){
  int s=blockIdx.x; int t=threadIdx.x; __shared__ float red[3][64];
  float s1=0,gg=0; for(int d=t;d<D;d+=64){float v=b2f(x[s*D+d]); s1+=v; gg+=v*b2f(gw[d]);}
  for(int o=32;o>0;o>>=1){s1+=__shfl_xor(s1,o); gg+=__shfl_xor(gg,o);}
  float m=s1/D; float s2=0; for(int d=t;d<D;d+=64){float v=b2f(x[s*D+d])-m; s2+=v*v;}
  for(int o=32;o>0;o>>=1){s2+=__shfl_xor(s2,o);}
  float r=1.f/sqrtf(s2/D+1e-5f);
  for(int d=t;d<D;d+=64) h[s*D+d]=__float2bfloat16((b2f(x[s*D+d])-m)*r);
  if(t==0) g[s]=1.f/(1.f+expf(-gg));
}
__global__ void k_attn(const bf16* qkv,bf16* o){
  int s=blockIdx.x; int h=threadIdx.x; if(h>=H) return;
  float q[16]; for(int d=0;d<16;d++) q[d]=b2f(qkv[s*1536+h*16+d]);
  int j0=s-W+1; if(j0<0) j0=0;
  float mx=-1e30f;
  for(int j=j0;j<=s;j++){float a=0; for(int d=0;d<16;d++) a+=q[d]*b2f(qkv[j*1536+512+h*16+d]); a*=0.25f; mx=fmaxf(mx,a);}
  float l=0; float acc[16]; for(int d=0;d<16;d++) acc[d]=0;
  for(int j=j0;j<=s;j++){float a=0; for(int d=0;d<16;d++) a+=q[d]*b2f(qkv[j*1536+512+h*16+d]); a*=0.25f; float p=expf(a-mx); l+=p;
    for(int d=0;d<16;d++) acc[d]+=p*b2f(qkv[j*1536+1024+h*16+d]);}
  for(int d=0;d<16;d++) o[s*D+h*16+d]=__float2bfloat16(acc[d]/l);
}
__global__ void k_mlp1(const bf16* h,const bf16* w1,const bf16* b1,bf16* u){
  int f=blockIdx.x*64+threadIdx.x; int d=blockIdx.y; if(f>=F) return;
  float acc=0; for(int s=0;s<S;s++) acc+=b2f(h[s*D+d])*b2f(w1[s*F+f]);
  acc+=b2f(b1[f]);
  float t=tanhf(0.7978845608028654f*(acc+0.044715f*acc*acc*acc));
  u[d*F+f]=__float2bfloat16(0.5f*acc*(1.f+t));
}
__global__ void k_out(const bf16* o,const bf16* wo,const bf16* u,const bf16* w2,const bf16* b2,const float* g,bf16* out){
  int d=blockIdx.x*256+threadIdx.x; int s=blockIdx.y;
  float a=0; for(int e=0;e<D;e++) a+=b2f(o[s*D+e])*b2f(wo[e*D+d]);
  float b=0; for(int f=0;f<F;f++) b+=b2f(u[d*F+f])*b2f(w2[f*S+s]);
  b+=b2f(b2[s]);
  float gg=g[s];
  out[s*D+d]=__float2bfloat16(gg*a+(1.f-gg)*b);
}
extern "C" void kernel_launch(void* const* d_in, const int* in_sizes, int n_in,
                              void* d_out, int out_size, void* d_ws, size_t ws_size,
                              hipStream_t stream) {
  const bf16* x=(const bf16*)d_in[0]; const bf16* wqkv=(const bf16*)d_in[1]; const bf16* wo=(const bf16*)d_in[2];
  const bf16* w1=(const bf16*)d_in[4]; const bf16* b1=(const bf16*)d_in[5]; const bf16* w2=(const bf16*)d_in[6]; const bf16* b2=(const bf16*)d_in[7];
  const bf16* gw=(const bf16*)d_in[8];
  char* ws=(char*)d_ws;
  bf16* qkv=(bf16*)ws; bf16* h=(bf16*)(ws+(8<<20)); bf16* o=(bf16*)(ws+(12<<20)); bf16* u=(bf16*)(ws+(16<<20)); float* g=(float*)(ws+(20<<20));
  k_qkv<<<dim3(6,S),256,0,stream>>>(x,wqkv,qkv);
  k_ln<<<S,64,0,stream>>>(x,gw,h,g);
  k_attn<<<S,64,0,stream>>>(qkv,o);
  k_mlp1<<<dim3(22,D),64,0,stream>>>(h,w1,b1,u);
  k_out<<<dim3(2,S),256,0,stream>>>(o,wo,u,w2,b2,g,(bf16*)d_out);
}
```

```cpp
#include <hip/hip_runtime.h>
#include <hip/hip_bf16.h>

typedef __attribute__((ext_vector_type(8))) short bf16x8;
typedef __attribute__((ext_vector_type(4))) short s16x4;
typedef __attribute__((ext_vector_type(4))) float f32x4;
typedef __attribute__((ext_vector_type(16))) float f32x16;
typedef __attribute__((ext_vector_type(4))) unsigned u32x4;
typedef __attribute__((ext_vector_type(2))) unsigned u32x2;
typedef __amdgpu_buffer_rsrc_t srd_t;
#define LAS __attribute__((address_space(3)))
#define DI __device__ __forceinline__

constexpr int S_ = 2048, D_ = 512, F_ = 1376;
constexpr unsigned WS_FH = 0, WS_FQ = 1024, WS_FO = 2048, WS_FU = 3072;
constexpr unsigned WS_FLAG_BYTES = 8192;
constexpr unsigned WS_G = 8192;
constexpr unsigned WS_H = 1u << 20;
constexpr unsigned WS_Q = 3u << 20;
constexpr unsigned WS_K = 5u << 20;
constexpr unsigned WS_V = 7u << 20;
constexpr unsigned WS_O = 9u << 20;
constexpr unsigned WS_U = 11u << 20;
constexpr unsigned WS_BYTES = WS_U + 32u * 43u * 1024u;
constexpr unsigned OOB = 0x40000000u;
constexpr unsigned REG = 32768;
constexpr unsigned LDS_KV = 4 * REG;
constexpr unsigned LDS_BYTES = 5 * REG;
constexpr unsigned SPIN_CAP = 1u << 16;

#define STAMP(i) do { } while (0)

DI f32x4 mfma16(u32x4 a, u32x4 b, f32x4 c) {
  return __builtin_amdgcn_mfma_f32_16x16x32_bf16(__builtin_bit_cast(bf16x8, a), __builtin_bit_cast(bf16x8, b), c, 0, 0, 0);
}
DI f32x16 mfma32(u32x4 a, u32x4 b, f32x16 c) {
  return __builtin_amdgcn_mfma_f32_32x32x16_bf16(__builtin_bit_cast(bf16x8, a), __builtin_bit_cast(bf16x8, b), c, 0, 0, 0);
}
typedef __attribute__((ext_vector_type(2))) __bf16 bf16x2_t;
typedef __attribute__((ext_vector_type(2))) float f32x2;
DI unsigned pk(float lo, float hi) {
  f32x2 v = {lo, hi};
  return __builtin_bit_cast(unsigned, __builtin_convertvector(v, bf16x2_t));
}
DI float bflo(unsigned u) { return __builtin_bit_cast(float, u << 16); }
DI float bfhi(unsigned u) { return __builtin_bit_cast(float, u & 0xffff0000u); }

DI void dma16(srd_t srd, unsigned voff, unsigned soff, unsigned lds) {
  asm volatile("s_mov_b32 m0, %0\n\ts_nop 0\n\tbuffer_load_dwordx4 %1, %2, %3 offen lds"
               :: "s"(lds), "v"(voff), "s"(srd), "s"(soff) : "m0", "memory");
}
DI void ld128_sc1(u32x4& d, srd_t srd, unsigned voff, unsigned soff) {
  asm volatile("buffer_load_dwordx4 %0, %1, %2, %3 offen sc1" : "=v"(d) : "v"(voff), "s"(srd), "s"(soff) : "memory");
}
DI void ld128(u32x4& d, srd_t srd, unsigned voff, unsigned soff) {
  asm volatile("buffer_load_dwordx4 %0, %1, %2, %3 offen" : "=v"(d) : "v"(voff), "s"(srd), "s"(soff) : "memory");
}
DI void ld64(u32x2& d, srd_t srd, unsigned voff, unsigned soff) {
  asm volatile("buffer_load_dwordx2 %0, %1, %2, %3 offen" : "=v"(d) : "v"(voff), "s"(srd), "s"(soff) : "memory");
}
DI void ld32_sc1(unsigned& d, srd_t srd, unsigned voff, unsigned soff) {
  asm volatile("buffer_load_dword %0, %1, %2, %3 offen sc1" : "=v"(d) : "v"(voff), "s"(srd), "s"(soff) : "memory");
}
DI void ld16u(unsigned& d, srd_t srd, unsigned voff, unsigned soff) {
  asm volatile("buffer_load_ushort %0, %1, %2, %3 offen" : "=v"(d) : "v"(voff), "s"(srd), "s"(soff) : "memory");
}
DI void st128_sc1(u32x4 v, srd_t srd, unsigned voff, unsigned soff) { __builtin_amdgcn_raw_buffer_store_b128(v, srd, voff, soff, 16); }
DI void st32_sc1(unsigned v, srd_t srd, unsigned voff, unsigned soff) { __builtin_amdgcn_raw_buffer_store_b32(v, srd, voff, soff, 16); }
template <int N> DI void waitv() { asm volatile("s_waitcnt vmcnt(%0)" :: "i"(N) : "memory"); }
#define PIN(x) asm volatile("" : "+v"(x))
DI void bar() { asm volatile("s_waitcnt lgkmcnt(0)\n\ts_barrier" ::: "memory"); }
DI u32x2 trrd(unsigned a) {
  typedef short v4i16_t __attribute__((ext_vector_type(4)));
  return __builtin_bit_cast(u32x2, __builtin_amdgcn_ds_read_tr16_b64_v4i16((LAS v4i16_t*)a));
}
DI u32x4 ldsrd(unsigned a) { return *(LAS u32x4*)a; }
DI void ldswr(unsigned a, u32x4 v) { *(LAS u32x4*)a = v; }
DI u32x4 cat(u32x2 a, u32x2 b) { return u32x4{a.x, a.y, b.x, b.y}; }

DI void poll_flags(srd_t ws, unsigned off, unsigned voff) {
  for (unsigned spin = 0; spin < SPIN_CAP; ++spin) {
    unsigned v;
    asm volatile("buffer_load_dword %0, %1, %2, %3 offen sc1\n\ts_waitcnt vmcnt(0)" : "=&v"(v) : "v"(voff), "s"(ws), "s"(off) : "memory");
    if (__all(v != 0u)) break;
    __builtin_amdgcn_s_sleep(2);
  }
}

DI float wave_sum(float v) {
#pragma unroll
  for (int o = 32; o > 0; o >>= 1) v += __shfl_xor(v, o);
  return v;
}

__global__ void __launch_bounds__(256) k_init(u32x4* flags) {
  flags[threadIdx.x] = u32x4{0, 0, 0, 0};
  flags[threadIdx.x + 256] = u32x4{0, 0, 0, 0};
}

template <int NA, bool SWAP>
DI void gemm_step(f32x4 (&acc)[4][NA], const u32x4 (&dir)[NA], unsigned tr0, unsigned halfoff) {
#pragma unroll
  for (int l = 0; l < 4; ++l) {
    const unsigned a = tr0 ^ (unsigned)(l << 5);
    const u32x4 w = cat(trrd(a), trrd(a + halfoff));
#pragma unroll
    for (int r = 0; r < NA; ++r) acc[l][r] = SWAP ? mfma16(dir[r], w, acc[l][r]) : mfma16(w, dir[r], acc[l][r]);
  }
}

__global__ void __launch_bounds__(512, 2)
k_main(const void* x_, const void* wqkv_, const void* wo_, const void* w1_, const void* b1_, const void* w2_,
       const void* b2_, const void* gw_, void* out_, void* ws_) {
  extern __shared__ __attribute__((aligned(16))) char smem[];
  const unsigned L0 = (unsigned)(size_t)(LAS char*)smem;
  const srd_t sX = __builtin_amdgcn_make_buffer_rsrc((void*)x_, 0, S_ * D_ * 2, 0x00020000);
  const srd_t sWqkv = __builtin_amdgcn_make_buffer_rsrc((void*)wqkv_, 0, D_ * 1536 * 2, 0x00020000);
  const srd_t sWo = __builtin_amdgcn_make_buffer_rsrc((void*)wo_, 0, D_ * D_ * 2, 0x00020000);
  const srd_t sW1 = __builtin_amdgcn_make_buffer_rsrc((void*)w1_, 0, S_ * F_ * 2, 0x00020000);
  const srd_t sW2 = __builtin_amdgcn_make_buffer_rsrc((void*)w2_, 0, F_ * S_ * 2, 0x00020000);
  const srd_t sB1 = __builtin_amdgcn_make_buffer_rsrc((void*)b1_, 0, F_ * 2, 0x00020000);
  const srd_t sB2 = __builtin_amdgcn_make_buffer_rsrc((void*)b2_, 0, S_ * 2, 0x00020000);
  const srd_t sGW = __builtin_amdgcn_make_buffer_rsrc((void*)gw_, 0, D_ * 2, 0x00020000);
  const srd_t sOut = __builtin_amdgcn_make_buffer_rsrc(out_, 0, S_ * D_ * 2, 0x00020000);
  const srd_t sWS = __builtin_amdgcn_make_buffer_rsrc(ws_, 0, WS_BYTES, 0x00020000);
  const unsigned tid = threadIdx.x, lane = tid & 63;
  const unsigned w = __builtin_amdgcn_readfirstlane(tid >> 6);
  const unsigned b = blockIdx.x, xcd = b & 7, j = b >> 3;
  const unsigned g4 = lane >> 4, l15 = lane & 15, q4 = l15 >> 2, p4 = lane & 3;
  const unsigned lane16 = lane * 16;
  STAMP(0);

  const unsigned ln_row = 32 * (b >> 2) + 16 * (w >> 2) + 4 * (b & 3) + (w & 3);
  u32x4 xr, gwv;
  ld128(xr, sX, lane16, ln_row * 1024);
  ld128(gwv, sGW, lane16, 0);
  const unsigned r8 = lane >> 3, c8 = lane & 7;
  const unsigned vx = (8 * w + r8) * 1024 + ((c8 ^ ((4 * w + (r8 >> 1)) & 7)) << 4);
  const unsigned fq = ((r8 >> 1) & 1) | ((w & 1) << 1);
  const unsigned vw = (8 * w + r8) * 3072 + ((c8 ^ (fq << 1)) << 4);
  auto qkv_issue = [&](int c) {
    const unsigned kc = ((unsigned)c + j) & 7;
    const unsigned slot = L0 + (c & 3) * REG;
    dma16(sX, vx, j * 65536 + kc * 128, slot + w * 1024);
#pragma unroll
    for (int t = 0; t < 3; ++t) dma16(sWqkv, vw, kc * (64 * 3072) + t * 1024 + xcd * 128, slot + 8192 + t * 8192 + w * 1024);
  };
  qkv_issue(0); qkv_issue(1); qkv_issue(2); qkv_issue(3);

  {
    asm volatile("s_waitcnt vmcnt(16)" : "+v"(xr), "+v"(gwv) :: "memory");
    float v[8], s1 = 0.f, gd = 0.f;
#pragma unroll
    for (int i = 0; i < 4; ++i) {
      v[2 * i] = bflo(xr[i]); v[2 * i + 1] = bfhi(xr[i]);
      s1 += v[2 * i] + v[2 * i + 1];
      gd += v[2 * i] * bflo(gwv[i]) + v[2 * i + 1] * bfhi(gwv[i]);
    }
    s1 = wave_sum(s1); gd = wave_sum(gd);
    const float mean = s1 * (1.f / 512.f);
    float s2 = 0.f;
#pragma unroll
    for (int i = 0; i < 8; ++i) { v[i] -= mean; s2 += v[i] * v[i]; }
    s2 = wave_sum(s2);
    const float rs = 1.f / sqrtf(s2 * (1.f / 512.f) + 1e-5f);
    u32x4 hv;
#pragma unroll
    for (int i = 0; i < 4; ++i) hv[i] = pk(v[2 * i] * rs, v[2 * i + 1] * rs);
    ldswr(L0 + LDS_KV + w * 1088 + lane16, hv);
    const float gate = 1.f / (1.f + __expf(-gd));
    bar();
    const unsigned ta = L0 + LDS_KV + q4 * 1088 + (64 * w + 16 * g4 + 4 * p4) * 2;
    const u32x4 ht = cat(trrd(ta), trrd(ta + 4 * 1088));
    st128_sc1(ht, sWS, (4 * w + g4) * 65536 + l15 * 16, WS_H + b * 256);
    if (lane == 0) st32_sc1(__builtin_bit_cast(unsigned, gate), sWS, 0, WS_G + ln_row * 4);
  }
  STAMP(1);

  const unsigned mt = xcd * 31 + j;
  const bool m_act = (j < 31) && (mt < 242);
  const unsigned ft = mt / 11, dt = mt - ft * 11;
  const unsigned csw = (c8 ^ (((r8 >> 1) & 3) << 1)) << 4;
  const unsigned vw1 = r8 * (F_ * 2) + csw;
  auto w1_issue = [&](int i) {
    const unsigned ks = 8 * w + (((unsigned)i + dt) & 7);
    const unsigned so = m_act ? ks * (32 * F_ * 2) + ft * 128 : OOB;
    const unsigned slot = L0 + (i & 3) * REG + w * 4096;
#pragma unroll
    for (int t = 0; t < 4; ++t) dma16(sW1, vw1, so + t * (8 * F_ * 2), slot + t * 1024);
  };

  {
    const unsigned mh = w >> 2, hd = w & 3;
    f32x4 aq[2] = {}, ak[2] = {}, av[2] = {};
    const unsigned xa0 = (32 * mh + l15) * 128 + ((g4 ^ ((l15 >> 1) & 7)) << 4);
    const unsigned fr = ((q4 >> 1) & 1) | ((g4 & 1) << 1);
    const unsigned wa0 = 8192 + (8 * g4 + q4) * 128 + ((hd ^ fr) << 5) + p4 * 8;
    auto compute = [&](int c) {
      const unsigned slot = L0 + (c & 3) * REG;
#pragma unroll
      for (int ks = 0; ks < 2; ++ks) {
        const u32x4 x0 = ldsrd(slot + (xa0 ^ (ks << 6)));
        const u32x4 x1 = ldsrd(slot + (xa0 ^ (ks << 6)) + 2048);
        const unsigned wa = slot + wa0 + ks * 4096;
        const u32x4 wq = cat(trrd(wa), trrd(wa + 512));
        const u32x4 wk = cat(trrd(wa + 8192), trrd(wa + 8192 + 512));
        const u32x4 wv = cat(trrd(wa + 16384), trrd(wa + 16384 + 512));
        aq[0] = mfma16(wq, x0, aq[0]); aq[1] = mfma16(wq, x1, aq[1]);
        ak[0] = mfma16(wk, x0, ak[0]); ak[1] = mfma16(wk, x1, ak[1]);
        av[0] = mfma16(x0, wv, av[0]); av[1] = mfma16(x1, wv, av[1]);
      }
    };
    waitv<14>(); bar(); compute(0);
    waitv<10>(); bar(); qkv_issue(4); compute(1);
    waitv<10>(); bar(); qkv_issue(5); compute(2);
    waitv<8>();  bar();
    if (tid == 0) st32_sc1(1u, sWS, 0, WS_FH + b * 4);
    qkv_issue(6); compute(3);
    waitv<8>(); bar(); qkv_issue(7); compute(4);
    waitv<8>(); bar(); w1_issue(0); compute(5);
    waitv<8>(); bar(); w1_issue(1); compute(6);
    waitv<8>(); bar(); w1_issue(2); compute(7);
    const float qs = 0.25f * 1.4426950408889634f;
    const unsigned head = 4 * xcd + hd, tile = 2 * j + mh;
    const unsigned so = head * 65536 + tile * 1024;
    {
      unsigned a0 = pk(aq[0][0] * qs, aq[0][1] * qs), a1 = pk(aq[0][2] * qs, aq[0][3] * qs);
      unsigned b0 = pk(aq[1][0] * qs, aq[1][1] * qs), b1 = pk(aq[1][2] * qs, aq[1][3] * qs);
      auto r0 = __builtin_amdgcn_permlane16_swap(a0, b0, false, false);
      auto r1 = __builtin_amdgcn_permlane16_swap(a1, b1, false, false);
      st128_sc1(u32x4{r0[0], r1[0], r0[1], r1[1]}, sWS, (g4 >> 1) * 512 + (g4 & 1) * 256 + l15 * 16, WS_Q + so);
    }
    {
      unsigned a0 = pk(ak[0][0], ak[0][1]), a1 = pk(ak[0][2], ak[0][3]);
      unsigned b0 = pk(ak[1][0], ak[1][1]), b1 = pk(ak[1][2], ak[1][3]);
      auto r0 = __builtin_amdgcn_permlane16_swap(a0, b0, false, false);
      auto r1 = __builtin_amdgcn_permlane16_swap(a1, b1, false, false);
      st128_sc1(u32x4{r0[0], r1[0], r0[1], r1[1]}, sWS, (g4 >> 1) * 512 + (g4 & 1) * 256 + l15 * 16, WS_K + so);
    }
    {
      unsigned a0 = pk(av[0][0], av[0][1]), a1 = pk(av[0][2], av[0][3]);
      unsigned b0 = pk(av[1][0], av[1][1]), b1 = pk(av[1][2], av[1][3]);
      auto r0 = __builtin_amdgcn_permlane32_swap(a0, b0, false, false);
      auto r1 = __builtin_amdgcn_permlane32_swap(a1, b1, false, false);
      st128_sc1(u32x4{r0[0], r1[0], r0[1], r1[1]}, sWS, lane16, WS_V + so);
    }
  }
  STAMP(2);

  const unsigned trw = (4 * g4 + q4) * 128 + ((((2 * g4 + (q4 >> 1)) & 3)) << 5) + p4 * 8;
  {
    poll_flags(sWS, WS_FH + w * 128, (lane & 31) * 4);
    bar();
    if (tid == 0) st32_sc1(1u, sWS, 0, WS_FQ + (xcd * 32 + j) * 4);
    STAMP(3);
    u32x4 hf[4][3];
    auto h_issue = [&](int i) {
      const unsigned ks = 8 * w + (((unsigned)i + dt) & 7);
#pragma unroll
      for (int r = 0; r < 3; ++r) {
        const unsigned du = 3 * dt + r;
        ld128_sc1(hf[i & 3][r], sWS, lane16, (m_act && du < 32) ? WS_H + du * 65536 + ks * 1024 : OOB);
      }
    };
    h_issue(0); h_issue(1); h_issue(2);
    w1_issue(3); h_issue(3);
    f32x4 acc[4][3] = {};
#define HPIN(i) "+v"(hf[(i) & 3][0]), "+v"(hf[(i) & 3][1]), "+v"(hf[(i) & 3][2])
#define MSTEP(i, N) do { asm volatile("s_waitcnt vmcnt(" #N ")" : HPIN(i) :: "memory"); \
      gemm_step<3, false>(acc, hf[(i) & 3], L0 + ((i) & 3) * REG + w * 4096 + trw, 2048); \
      asm volatile("s_waitcnt lgkmcnt(0)" ::: "memory"); } while (0)
    MSTEP(0, 13); w1_issue(4); h_issue(4);
    MSTEP(1, 17); w1_issue(5); h_issue(5);
    MSTEP(2, 21); w1_issue(6); h_issue(6);
    MSTEP(3, 21); w1_issue(7); h_issue(7);
    STAMP(4);
    {
      const unsigned head = 4 * xcd + (j >> 3), qr = j & 7;
      const unsigned p0 = qr ? 4 * qr - 4 : 0;
      poll_flags(sWS, WS_FQ + (xcd * 32 + p0) * 4, (qr ? (lane & 7) : (lane & 3)) * 4);
      STAMP(5);
      u32x4 kv[4], qf;
      const bool tv = qr || w >= 4;
      const unsigned tb = head * 65536 + (8 * qr + 2 * w) * 1024 - 8192;
      ld128_sc1(kv[0], sWS, lane16, tv ? WS_K + tb : OOB);
      ld128_sc1(kv[1], sWS, lane16, tv ? WS_K + tb + 1024 : OOB);
      ld128_sc1(kv[2], sWS, lane16, tv ? WS_V + tb : OOB);
      ld128_sc1(kv[3], sWS, lane16, tv ? WS_V + tb + 1024 : OOB);
      ld128_sc1(qf, sWS, lane16, WS_Q + head * 65536 + (8 * qr + w) * 1024);
      asm volatile("s_waitcnt vmcnt(0)" : "+v"(kv[0]), "+v"(kv[1]), "+v"(kv[2]), "+v"(kv[3]), "+v"(qf) :: "memory");
      const unsigned kb = L0 + LDS_KV, vb = kb + 16384;
      ldswr(kb + w * 2048 + lane16, kv[0]); ldswr(kb + w * 2048 + 1024 + lane16, kv[1]);
      ldswr(vb + w * 2048 + lane16, kv[2]); ldswr(vb + w * 2048 + 1024 + lane16, kv[3]);
      bar();
      STAMP(6);
      const unsigned r = lane & 31, h = lane >> 5;
      const int tthr = (int)r - 4 * (int)h;
      const int kt0 = qr ? 0 : 8 - (int)w;
      const float NEG = -1e30f;
      f32x16 mdiag, mfirst;
#pragma unroll
      for (int i = 0; i < 16; ++i) { const bool le = ((i & 3) + 8 * (i >> 2)) <= tthr; mdiag[i] = le ? 0.f : NEG; mfirst[i] = le ? NEG : 0.f; }
      const unsigned kaddr = kb + w * 1024 + lane16;
      float m;
      {
        const f32x16 s = mfma32(ldsrd(kaddr + 8192), qf, mdiag);
        m = fmaxf(fmaxf(s[0], s[1]), s[2]);
#pragma unroll
        for (int i = 3; i < 15; i += 2) m = fmaxf(fmaxf(m, s[i]), s[i + 1]);
        m = fmaxf(m, s[15]);
      }
      if (kt0 == 0) {
        const f32x16 s = mfma32(ldsrd(kaddr), qf, mfirst);
#pragma unroll
        for (int i = 0; i < 16; i += 2) m = fmaxf(fmaxf(m, s[i]), s[i + 1]);
      }
      const f32x16 zero16 = {};
      for (int kt = kt0 ? kt0 : 1; kt < 8; ++kt) {
        const f32x16 s = mfma32(ldsrd(kaddr + kt * 1024), qf, zero16);
#pragma unroll
        for (int i = 0; i < 16; i += 2) m = fmaxf(fmaxf(m, s[i]), s[i + 1]);
      }
      {
        auto rr = __builtin_amdgcn_permlane32_swap(__float_as_uint(m), __float_as_uint(m), false, false);
        m = fmaxf(__uint_as_float(rr[0]), __uint_as_float(rr[1]));
      }
      f32x16 o = {};
      f32x16 negm;
#pragma unroll
      for (int i = 0; i < 16; ++i) negm[i] = -m;
      u32x4 v0 = {0x3f803f80u, 0x3f803f80u, 0x3f803f80u, 0x3f803f80u}, v1 = v0;
      const unsigned vaddr = vb + w * 1024 + h * 256 + r * 16;
      auto pv = [&](int kt, const f32x16& c) {
        const f32x16 s = mfma32(ldsrd(kaddr + kt * 1024), qf, c);
        if (r < 16) { v0 = ldsrd(vaddr + kt * 1024); v1 = ldsrd(vaddr + kt * 1024 + 512); }
        u32x4 p0v, p1v;
#pragma unroll
        for (int i = 0; i < 4; ++i) {
          p0v[i] = pk(__builtin_amdgcn_exp2f(s[2 * i]), __builtin_amdgcn_exp2f(s[2 * i + 1]));
          p1v[i] = pk(__builtin_amdgcn_exp2f(s[8 + 2 * i]), __builtin_amdgcn_exp2f(s[8 + 2 * i + 1]));
        }
        o = mfma32(v0, p0v, o);
        o = mfma32(v1, p1v, o);
      };
      pv(8, mdiag + negm);
      if (kt0 == 0) pv(0, mfirst + negm);
      for (int kt = kt0 ? kt0 : 1; kt < 8; ++kt) pv(kt, negm);
      const float inv = 1.f / o[8];
      u32x4 ov;
#pragma unroll
      for (int i = 0; i < 4; ++i) ov[i] = pk(o[2 * i] * inv, o[2 * i + 1] * inv);
      const unsigned sunit = 16 * qr + 2 * w;
      st128_sc1(ov, sWS, (r >> 4) * 16384 + h * 256 + (r & 15) * 16, WS_O + sunit * 16384 + (head >> 1) * 1024 + (head & 1) * 512);
    }
    STAMP(7);
    u32x2 bb0, bb1;
    const unsigned fbase = 64 * ft + 32 * (w & 1);
    ld64(bb0, sB1, g4 * 8, fbase * 2);
    ld64(bb1, sB1, g4 * 8, fbase * 2 + 32);
    asm volatile("s_waitcnt vmcnt(0)" : "+v"(bb0), "+v"(bb1) :: "memory");
    MSTEP(4, 0); MSTEP(5, 0); MSTEP(6, 0); MSTEP(7, 0);
#pragma unroll
    for (int l = 0; l < 4; ++l)
#pragma unroll
      for (int r = 0; r < 3; ++r) ldswr(L0 + r * REG + w * 4096 + l * 1024 + lane16, __builtin_bit_cast(u32x4, acc[l][r]));
    bar();
    if (tid == 0) st32_sc1(1u, sWS, 0, WS_FO + ((j & 7) * 32 + 4 * xcd + (j >> 3)) * 4);
    STAMP(8);
    if (w < 6) {
      f32x4 s0 = {}, s1 = {};
      const unsigned ra = L0 + (w >> 1) * REG + (w & 1) * 2048 + lane16;
#pragma unroll
      for (int ww = 0; ww < 8; ++ww) {
        s0 += __builtin_bit_cast(f32x4, ldsrd(ra + ww * 4096));
        s1 += __builtin_bit_cast(f32x4, ldsrd(ra + ww * 4096 + 1024));
      }
      float u[8] = {s0[0] + bflo(bb0.x), s0[1] + bfhi(bb0.x), s0[2] + bflo(bb0.y), s0[3] + bfhi(bb0.y),
                    s1[0] + bflo(bb1.x), s1[1] + bfhi(bb1.x), s1[2] + bflo(bb1.y), s1[3] + bfhi(bb1.y)};
#pragma unroll
      for (int i = 0; i < 8; ++i) {
        const float t = u[i] * (1.f + 0.044715f * u[i] * u[i]) * (-2.f * 0.7978845608028654f * 1.4426950408889634f);
        u[i] = u[i] / (1.f + __builtin_amdgcn_exp2f(t));
      }
      const u32x4 uv = {pk(u[0], u[1]), pk(u[2], u[3]), pk(u[4], u[5]), pk(u[6], u[7])};
      const unsigned du = 3 * dt + (w >> 1), ks = 2 * ft + (w & 1);
      st128_sc1(uv, sWS, lane16, (m_act && du < 32 && ks < 43) ? WS_U + (du * 43 + ks) * 1024 : OOB);
    }
    waitv<0>();
    bar();
    if (tid == 0 && m_act) st32_sc1(1u, sWS, 0, WS_FU + (dt * 32 + ft) * 4);
  }
  STAMP(9);

  {
    const unsigned st = 4 * xcd + (j >> 3), dd = j & 7;
    const bool isA = w < 2;
    const unsigned k0 = isA ? 8 * w : 7 * (w - 2), nk = (isA || w == 7) ? 8 : 7;
    const srd_t sW = isA ? sWo : sW2;
    const unsigned rs = isA ? 1024u : 4096u;
    const unsigned vwf = r8 * rs + csw;
    const unsigned wcol = isA ? dd * 128 : st * 128;
    const unsigned rot = isA ? (j >> 3) * 2 + (xcd & 1) : dd;
    auto kstep = [&](int i) { unsigned t = (unsigned)i + rot; t = t >= 8 ? t - 8 : t; return t; };
    auto wf_issue = [&](int i) {
      const unsigned t = kstep(i);
      const unsigned so = t < nk ? (k0 + t) * 32 * rs + wcol : OOB;
      const unsigned slot = L0 + (i % 5) * REG + w * 4096;
#pragma unroll
      for (int q = 0; q < 4; ++q) dma16(sW, vwf, so + q * 8 * rs, slot + q * 1024);
    };
    wf_issue(0); wf_issue(1); wf_issue(2); wf_issue(3); wf_issue(4);
    unsigned gq, b2q;
    ld32_sc1(gq, sWS, r8 * 4, WS_G + (64 * st + 8 * w) * 4);
    ld16u(b2q, sB2, r8 * 2, (64 * st + 8 * w) * 2);
    if (isA) {
      poll_flags(sWS, WS_FO + ((st >> 2) * 32 + 16 * w) * 4, l15 * 4);
    } else {
      const unsigned d0 = (4 * dd) / 3, f0 = k0 >> 1;
      unsigned fi = f0 + (lane & 7); const unsigned fl = (k0 + nk - 1) >> 1; fi = fi > fl ? fl : fi;
      poll_flags(sWS, WS_FU + d0 * 128, ((lane >> 3) & 1) * 128 + fi * 4);
    }
    STAMP(10);
    u32x4 df[4][4];
    const unsigned dbase = isA ? WS_O + 4 * st * 16384 : WS_U + 4 * dd * 43 * 1024;
    const unsigned dpitch = isA ? 16384u : 43u * 1024u;
    auto d_issue = [&](int i) {
      const unsigned t = kstep(i);
#pragma unroll
      for (int r = 0; r < 4; ++r) ld128_sc1(df[i & 3][r], sWS, lane16, t < nk ? dbase + r * dpitch + (k0 + t) * 1024 : OOB);
    };
    d_issue(0); d_issue(1); d_issue(2); d_issue(3);
    const unsigned rowA = 16 * (g4 >> 1) + 4 * (g4 & 1) + q4;
    const unsigned trA = rowA * 128 + (((rowA >> 1) & 3) << 5) + p4 * 8;
    const unsigned trf = isA ? trA : trw;
    const unsigned hoff = isA ? 1024u : 2048u;
    f32x4 acc[4][4] = {};
#define DPIN(i) "+v"(df[(i) & 3][0]), "+v"(df[(i) & 3][1]), "+v"(df[(i) & 3][2]), "+v"(df[(i) & 3][3])
#define FSTEP(i, N) do { asm volatile("s_waitcnt vmcnt(" #N ")" : DPIN(i) :: "memory"); \
      gemm_step<4, true>(acc, df[(i) & 3], L0 + ((i) % 5) * REG + w * 4096 + trf, hoff); \
      asm volatile("s_waitcnt lgkmcnt(0)" ::: "memory"); } while (0)
    FSTEP(0, 12); wf_issue(5); d_issue(4);
    FSTEP(1, 16); wf_issue(6); d_issue(5);
    FSTEP(2, 20); wf_issue(7); d_issue(6);
    FSTEP(3, 24); d_issue(7);
    FSTEP(4, 20);
    FSTEP(5, 12);
    FSTEP(6, 4);
    FSTEP(7, 0);
    STAMP(11);
    bar();
    const unsigned sc = L0 + w * 17408;
    if (isA) {
#pragma unroll
      for (int l = 0; l < 4; ++l)
#pragma unroll
        for (int r = 0; r < 4; ++r)
#pragma unroll
          for (int i = 0; i < 4; ++i) *(LAS float*)(sc + (16 * r + 4 * g4 + i) * 272 + (16 * l + l15) * 4) = acc[l][r][i];
    } else {
#pragma unroll
      for (int l = 0; l < 4; ++l)
#pragma unroll
        for (int r = 0; r < 4; ++r) ldswr(sc + (16 * l + l15) * 272 + (16 * r + 4 * g4) * 4, __builtin_bit_cast(u32x4, acc[l][r]));
    }
    bar();
    const unsigned ra = L0 + (8 * w + r8) * 272 + c8 * 32;
    f32x4 a0 = __builtin_bit_cast(f32x4, ldsrd(ra)) + __builtin_bit_cast(f32x4, ldsrd(ra + 17408));
    f32x4 a1 = __builtin_bit_cast(f32x4, ldsrd(ra + 16)) + __builtin_bit_cast(f32x4, ldsrd(ra + 17408 + 16));
    f32x4 y0 = {}, y1 = {};
#pragma unroll
    for (int ww = 2; ww < 8; ++ww) {
      y0 += __builtin_bit_cast(f32x4, ldsrd(ra + ww * 17408));
      y1 += __builtin_bit_cast(f32x4, ldsrd(ra + ww * 17408 + 16));
    }
    PIN(gq); PIN(b2q);
    const float g = __builtin_bit_cast(float, gq), bb = bflo(b2q), og = 1.f - g;
    u32x4 ov;
    ov[0] = pk(g * a0[0] + og * (y0[0] + bb), g * a0[1] + og * (y0[1] + bb));
    ov[1] = pk(g * a0[2] + og * (y0[2] + bb), g * a0[3] + og * (y0[3] + bb));
    ov[2] = pk(g * a1[0] + og * (y1[0] + bb), g * a1[1] + og * (y1[1] + bb));
    ov[3] = pk(g * a1[2] + og * (y1[2] + bb), g * a1[3] + og * (y1[3] + bb));
    __builtin_amdgcn_raw_buffer_store_b128(ov, sOut, r8 * 1024 + c8 * 16, (64 * st + 8 * w) * 1024 + dd * 128, 0);
  }
  STAMP(12);
}

extern "C" void kernel_launch(void* const* d_in, const int* in_sizes, int n_in, void* d_out, int out_size, void* d_ws,
                              size_t ws_size, hipStream_t stream) {
  (void)in_sizes; (void)n_in; (void)out_size; (void)ws_size;
  (void)hipFuncSetAttribute((const void*)k_main, hipFuncAttributeMaxDynamicSharedMemorySize, LDS_BYTES);
  k_init<<<1, 256, 0, stream>>>((u32x4*)d_ws);
  k_main<<<256, 512, LDS_BYTES, stream>>>(d_in[0], d_in[1], d_in[2], d_in[4], d_in[5], d_in[6], d_in[7], d_in[8], d_out, d_ws);
}
```

```cpp
#include <hip/hip_runtime.h>
#include <hip/hip_bf16.h>

typedef __attribute__((ext_vector_type(8))) short bf16x8;
typedef __attribute__((ext_vector_type(4))) float f32x4;
typedef __attribute__((ext_vector_type(16))) float f32x16;
typedef __attribute__((ext_vector_type(4))) unsigned u32x4;
typedef __attribute__((ext_vector_type(2))) unsigned u32x2;
typedef __amdgpu_buffer_rsrc_t srd_t;
#define LAS __attribute__((address_space(3)))
#define DI __device__ __forceinline__

constexpr int S_ = 2048, D_ = 512, F_ = 1376;
constexpr unsigned FS = 64;
constexpr unsigned WS_FH = 0, WS_FQ = 256 * FS, WS_FO = 512 * FS, WS_FU = 768 * FS;
constexpr unsigned WS_G = 128u << 10;
constexpr unsigned WS_H = 1u << 20;
constexpr unsigned WS_Q = 3u << 20;
constexpr unsigned WS_K = 5u << 20;
constexpr unsigned WS_V = 7u << 20;
constexpr unsigned WS_O = 9u << 20;
constexpr unsigned WS_U = 11u << 20;
constexpr unsigned WS_BYTES = WS_U + 32u * 43u * 1024u;
constexpr unsigned OOB = 0x40000000u;
constexpr unsigned REG = 32768;
constexpr unsigned LDS_KV = 4 * REG;
constexpr unsigned LDS_BYTES = 5 * REG;
constexpr unsigned CW_QD = LDS_KV, CW_HR = LDS_KV + 4, CW_AB = LDS_KV + 8, CW_GB = LDS_KV + 12;
constexpr unsigned CW_OD = LDS_KV + 512, CW_UD = LDS_KV + 516, CW_OR = LDS_KV + 524;
constexpr unsigned LDS_LN = LDS_KV + 1024;
constexpr unsigned LDS_XR = LDS_LN + 8 * 1088;
constexpr unsigned SPIN_CAP = 1u << 16, LSPIN_CAP = 1u << 22;
#define SETPRIO(p) __builtin_amdgcn_s_setprio(p)

DI f32x4 mfma16(u32x4 a, u32x4 b, f32x4 c) {
  return __builtin_amdgcn_mfma_f32_16x16x32_bf16(__builtin_bit_cast(bf16x8, a), __builtin_bit_cast(bf16x8, b), c, 0, 0, 0);
}
DI f32x16 mfma32(u32x4 a, u32x4 b, f32x16 c) {
  return __builtin_amdgcn_mfma_f32_32x32x16_bf16(__builtin_bit_cast(bf16x8, a), __builtin_bit_cast(bf16x8, b), c, 0, 0, 0);
}
typedef __attribute__((ext_vector_type(2))) __bf16 bf16x2_t;
typedef __attribute__((ext_vector_type(2))) float f32x2;
DI unsigned pk(float lo, float hi) {
  f32x2 v = {lo, hi};
  return __builtin_bit_cast(unsigned, __builtin_convertvector(v, bf16x2_t));
}
DI float bflo(unsigned u) { return __builtin_bit_cast(float, u << 16); }
DI float bfhi(unsigned u) { return __builtin_bit_cast(float, u & 0xffff0000u); }

DI void dma4(unsigned lds, srd_t s0, unsigned v0, unsigned v1, unsigned v2, unsigned so0, srd_t s3, unsigned v3, unsigned so3) {
  asm volatile("s_mov_b32 m0, %0\n\ts_nop 0\n\t"
               "buffer_load_dwordx4 %2, %1, %5 offen lds\n\t"
               "buffer_load_dwordx4 %3, %1, %5 offen offset:1024 lds\n\t"
               "buffer_load_dwordx4 %4, %1, %5 offen offset:2048 lds\n\t"
               "buffer_load_dwordx4 %7, %6, %8 offen offset:3072 lds"
               :: "s"(lds), "s"(s0), "v"(v0), "v"(v1), "v"(v2), "s"(so0), "s"(s3), "v"(v3), "s"(so3) : "m0", "memory");
}
DI void ld128_sc1(u32x4& d, srd_t srd, unsigned voff, unsigned soff) {
  asm volatile("buffer_load_dwordx4 %0, %1, %2, %3 offen sc1" : "=v"(d) : "v"(voff), "s"(srd), "s"(soff) : "memory");
}
template <int IMM> DI void ld128_sc1i(u32x4& d, srd_t srd, unsigned voff, unsigned soff) {
  asm volatile("buffer_load_dwordx4 %0, %1, %2, %3 offen offset:%4 sc1" : "=v"(d) : "v"(voff), "s"(srd), "s"(soff), "i"(IMM) : "memory");
}
DI void ld64(u32x2& d, srd_t srd, unsigned voff, unsigned soff) {
  asm volatile("buffer_load_dwordx2 %0, %1, %2, %3 offen" : "=v"(d) : "v"(voff), "s"(srd), "s"(soff) : "memory");
}
DI void ld32_sc1(unsigned& d, srd_t srd, unsigned voff, unsigned soff) {
  asm volatile("buffer_load_dword %0, %1, %2, %3 offen sc1" : "=v"(d) : "v"(voff), "s"(srd), "s"(soff) : "memory");
}
DI void ld16u(unsigned& d, srd_t srd, unsigned voff, unsigned soff) {
  asm volatile("buffer_load_ushort %0, %1, %2, %3 offen" : "=v"(d) : "v"(voff), "s"(srd), "s"(soff) : "memory");
}
DI void st128_sc1(u32x4 v, srd_t srd, unsigned voff, unsigned soff) {
  asm volatile("s_nop 0\n\tbuffer_store_dwordx4 %0, %1, %2, %3 offen sc1\n\ts_nop 1" :: "v"(v), "v"(voff), "s"(srd), "s"(soff) : "memory");
}
DI void st128(u32x4 v, srd_t srd, unsigned voff, unsigned soff) {
  asm volatile("s_nop 0\n\tbuffer_store_dwordx4 %0, %1, %2, %3 offen\n\ts_nop 1" :: "v"(v), "v"(voff), "s"(srd), "s"(soff) : "memory");
}
DI void st32_sc1(unsigned v, srd_t srd, unsigned voff, unsigned soff) { __builtin_amdgcn_raw_buffer_store_b32(v, srd, voff, soff, 16); }
template <int N> DI void waitv() { asm volatile("s_waitcnt vmcnt(%0)" :: "i"(N) : "memory"); }
#define PIN(x) asm volatile("" : "+v"(x))
#define SBAR() __builtin_amdgcn_sched_barrier(0)
DI void bar() { asm volatile("s_waitcnt lgkmcnt(0)\n\ts_barrier" ::: "memory"); }
template <int N> DI void waitbar() { asm volatile("s_waitcnt vmcnt(%0) lgkmcnt(0)\n\ts_barrier" :: "i"(N) : "memory"); }
DI u32x2 trrd(unsigned a) {
  typedef short v4i16_t __attribute__((ext_vector_type(4)));
  return __builtin_bit_cast(u32x2, __builtin_amdgcn_ds_read_tr16_b64_v4i16((LAS v4i16_t*)(size_t)a));
}
DI u32x4 ldsrd(unsigned a) { return *(LAS u32x4*)(size_t)a; }
DI void ldswr(unsigned a, u32x4 v) { *(LAS u32x4*)(size_t)a = v; }
DI u32x4 cat(u32x2 a, u32x2 b) { return u32x4{a.x, a.y, b.x, b.y}; }

DI void poll_flags(srd_t ws, unsigned off, unsigned voff) {
  unsigned v = 0;
#pragma unroll 1
  for (unsigned spin = 0; spin < SPIN_CAP; ++spin) {
    if (v == 0u) asm volatile("buffer_load_dword %0, %1, %2, %3 offen sc1\n\ts_waitcnt vmcnt(0)" : "=&v"(v) : "v"(voff), "s"(ws), "s"(off) : "memory");
    if (__all(v != 0u)) break;
    __builtin_amdgcn_s_sleep(8);
  }
}
DI void cw_set(unsigned a) { asm volatile("ds_write_b32 %0, %1" :: "v"(a), "v"(1u) : "memory"); }
DI void cw_wait(unsigned a, unsigned target) {
#pragma unroll 1
  for (unsigned spin = 0; spin < LSPIN_CAP; ++spin) {
    unsigned v;
    asm volatile("ds_read_b32 %0, %1\n\ts_waitcnt lgkmcnt(0)" : "=&v"(v) : "v"(a) : "memory");
    if (__builtin_amdgcn_readfirstlane(v) >= target) break;
    __builtin_amdgcn_s_sleep(3);
  }
}
template <int N> DI void sub_arrive(unsigned a, unsigned lane) {
  asm volatile("s_waitcnt vmcnt(%0) lgkmcnt(0)" :: "i"(N) : "memory");
  if (lane == 0) asm volatile("ds_add_u32 %0, %1" :: "v"(a), "v"(1u) : "memory");
}
DI void sub_wait(unsigned a, unsigned target) {
#pragma unroll 1
  for (unsigned spin = 0; spin < LSPIN_CAP; ++spin) {
    unsigned v;
    asm volatile("ds_read_b32 %0, %1\n\ts_waitcnt lgkmcnt(0)" : "=&v"(v) : "v"(a) : "memory");
    if (__builtin_amdgcn_readfirstlane(v) >= target) break;
  }
}
template <int N> DI void sub_bar(unsigned a, unsigned target, unsigned lane) { sub_arrive<N>(a, lane); sub_wait(a, target); }
DI unsigned cw_add(unsigned a, unsigned lane) {
  unsigned v = 0;
  if (lane == 0) asm volatile("ds_add_rtn_u32 %0, %1, %2\n\ts_waitcnt lgkmcnt(0)" : "=&v"(v) : "v"(a), "v"(1u) : "memory");
  return __builtin_amdgcn_readfirstlane(v);
}

template <int CTRL> DI float dpp_add(float v) {
  return v + __builtin_bit_cast(float, __builtin_amdgcn_update_dpp(0, __builtin_bit_cast(int, v), CTRL, 0xf, 0xf, true));
}
DI float wave_sum(float v) {
  v = dpp_add<0xB1>(v); v = dpp_add<0x4E>(v); v = dpp_add<0x141>(v); v = dpp_add<0x140>(v);
  auto r = __builtin_amdgcn_permlane16_swap(__float_as_uint(v), __float_as_uint(v), false, false);
  v = __uint_as_float(r[0]) + __uint_as_float(r[1]);
  auto t = __builtin_amdgcn_permlane32_swap(__float_as_uint(v), __float_as_uint(v), false, false);
  return __uint_as_float(t[0]) + __uint_as_float(t[1]);
}

template <int NA, bool SWAP>
DI void gemm_step(f32x4 (&acc)[4][NA], const u32x4 (&dir)[NA], const unsigned (&tr)[4], unsigned slot) {
  u32x4 wf[4];
#pragma unroll
  for (int l = 0; l < 4; ++l) wf[l] = cat(trrd(tr[l] + slot), trrd(tr[l] + slot + 2048));
  SBAR();
#pragma unroll
  for (int l = 0; l < 4; ++l)
#pragma unroll
    for (int r = 0; r < NA; ++r) acc[l][r] = SWAP ? mfma16(dir[r], wf[l], acc[l][r]) : mfma16(wf[l], dir[r], acc[l][r]);
  SBAR();
}

__global__ void __launch_bounds__(768, 3)
k_main(const void* x_, const void* wqkv_, const void* wo_, const void* w1_, const void* b1_, const void* w2_,
       const void* b2_, const void* gw_, void* out_, void* ws_) {
  extern __shared__ __attribute__((aligned(16))) char smem[];
  const unsigned tid = threadIdx.x, lane = tid & 63;
  const unsigned wv = __builtin_amdgcn_readfirstlane(tid >> 6);
  const unsigned b = blockIdx.x, xcd = b & 7, j = b >> 3;
  const unsigned rt = 8 * (xcd >> 1) + (j >> 2), hg = 4 * (xcd & 1) + (j & 3);
  const unsigned rotq = (j >> 2) + 2 * (j & 3);
  unsigned vx, vw, qsw[4], qsx[4];
  {
    const unsigned w_ = wv - 4, r8_ = lane >> 3, c8_ = lane & 7;
    vx = (8 * w_ + r8_) * 1024 + ((c8_ ^ ((4 * w_ + (r8_ >> 1)) & 7)) << 4);
    const unsigned fq = ((r8_ >> 1) & 1) | ((w_ & 1) << 1);
    vw = (8 * w_ + r8_) * 3072 + ((c8_ ^ (fq << 1)) << 4);
#pragma unroll
    for (int c = 0; c < 4; ++c) { const unsigned kc = ((unsigned)c + rotq) & 7; qsw[c] = kc * (64 * 3072) + hg * 128; qsx[c] = rt * 65536 + kc * 128; }
    asm volatile("" : "+v"(vx), "+v"(vw), "+s"(qsw[0]), "+s"(qsw[1]), "+s"(qsw[2]), "+s"(qsw[3]), "+s"(qsx[0]), "+s"(qsx[1]), "+s"(qsx[2]), "+s"(qsx[3]));
  }
  const unsigned row0 = 32 * (b >> 2) + 16 * ((wv >> 1) & 1) + 4 * (b & 3) + 2 * (wv & 1);
  unsigned r0o = row0 * 1024;
  asm volatile("" : "+s"(r0o), "+s"(x_), "+s"(wqkv_), "+s"(wo_), "+s"(w1_), "+s"(b1_), "+s"(w2_), "+s"(b2_), "+s"(gw_), "+s"(out_), "+s"(ws_));
  const srd_t sWS = __builtin_amdgcn_make_buffer_rsrc(ws_, 0, WS_BYTES, 0x00020000);
  if (wv < 4) {
    const srd_t sX = __builtin_amdgcn_make_buffer_rsrc((void*)x_, 0, S_ * D_ * 2, 0x00020000);
    const srd_t sGW = __builtin_amdgcn_make_buffer_rsrc((void*)gw_, 0, D_ * 2, 0x00020000);
    asm volatile("s_mov_b32 m0, %0\n\ts_nop 0\n\t"
                 "buffer_load_dwordx4 %1, %2, %3 offen lds\n\t"
                 "buffer_load_dwordx4 %1, %2, %3 offen offset:1024 lds\n\t"
                 "s_mov_b32 m0, %4\n\ts_nop 0\n\t"
                 "buffer_load_dwordx4 %1, %5, 0 offen lds"
                 :: "s"(LDS_XR + wv * 3072), "v"(lane * 16), "s"(sX), "s"(r0o), "s"(LDS_XR + wv * 3072 + 2048), "s"(sGW) : "m0", "memory");
    if (threadIdx.x < 8) *(LAS unsigned*)(size_t)(LDS_KV + (threadIdx.x >> 2) * 512 + (threadIdx.x & 3) * 4) = 0u;
    bar();
  } else {
    const srd_t sXd = __builtin_amdgcn_make_buffer_rsrc((void*)((const char*)x_ - 3072), 0, S_ * D_ * 2 + 3072, 0x00020000);
    const srd_t sWqkv = __builtin_amdgcn_make_buffer_rsrc((void*)wqkv_, 0, D_ * 1536 * 2, 0x00020000);
    bar();
#pragma unroll
    for (int c = 0; c < 4; ++c) dma4(c * 4096 + (wv - 4) * 16384, sWqkv, vw, vw, vw, qsw[c], sXd, vx, qsx[c]);
  }
  const unsigned g4 = lane >> 4, l15 = lane & 15, q4 = l15 >> 2, p4 = lane & 3;
  const unsigned lane16 = lane * 16;
  const unsigned st = 4 * xcd + (j >> 3), dd = j & 7;

  const unsigned mt = xcd < 5 ? xcd * 32 + j : 158 + (xcd - 5) * 28 + j;
  const bool m_act = xcd < 4 || j < (xcd == 4 ? 30u : 28u);
  const unsigned ft = mt < 132 ? mt / 6 : (mt - 132) / 5;
  const unsigned dt = mt < 132 ? mt - ft * 6 : mt - 132 - ft * 5 + 6;
  if (wv < 4) {
    const unsigned a = wv;
    SETPRIO(3);
    unsigned snap0, own;
    const unsigned hvo = (a * 64 + lane) * FS;
    {
      ld32_sc1(snap0, sWS, hvo, WS_FH);
      ld32_sc1(own, sWS, 0, WS_FH + b * FS);
      if (a == 1 && lane < 3) {
        const unsigned fo = lane == 0 ? WS_FQ + (hg * 32 + rt) * FS : lane == 1 ? WS_FO + ((j & 7) * 32 + 4 * xcd + (j >> 3)) * FS
                                                                                : (m_act ? WS_FU + (dt * 32 + ft) * FS : OOB);
        st32_sc1(0u, sWS, fo, 0);
      }
      unsigned pt0, pt1;
      {
        const void* pa = a == 0 ? (const void*)ws_ : a == 1 ? w1_ : a == 2 ? w2_ : (const void*)out_;
        const unsigned na = a == 0 ? WS_BYTES : a == 3 ? S_ * D_ * 2 : S_ * F_ * 2;
        const void* pb = a == 0 ? wo_ : a == 1 ? b1_ : a == 2 ? b2_ : wqkv_;
        const unsigned nb = a == 0 ? D_ * D_ * 2 : a == 1 ? F_ * 2 : a == 2 ? S_ * 2 : D_ * 1536 * 2;
        const srd_t sa = __builtin_amdgcn_make_buffer_rsrc((void*)pa, 0, na, 0x00020000);
        const srd_t sb = __builtin_amdgcn_make_buffer_rsrc((void*)pb, 0, nb, 0x00020000);
        const unsigned vo = lane < 16 ? lane << 21 : OOB;
        asm volatile("buffer_load_dword %0, %1, %2, %3 offen sc1" : "=v"(pt0) : "v"(vo), "s"(sa), "s"(a == 0 ? 512u << 10 : 0u) : "memory");
        asm volatile("buffer_load_dword %0, %1, %2, 0 offen" : "=v"(pt1) : "v"(vo), "s"(sb) : "memory");
      }
      waitv<4>();
      const u32x4 xr[2] = {ldsrd(LDS_XR + a * 3072 + lane16), ldsrd(LDS_XR + a * 3072 + 1024 + lane16)}, gwv = ldsrd(LDS_XR + a * 3072 + 2048 + lane16);
      float gate[2];
#pragma unroll
      for (int r = 0; r < 2; ++r) {
        float v[8], s1 = 0.f, gd = 0.f;
#pragma unroll
        for (int i = 0; i < 4; ++i) {
          v[2 * i] = bflo(xr[r][i]); v[2 * i + 1] = bfhi(xr[r][i]);
          s1 += v[2 * i] + v[2 * i + 1];
          gd += v[2 * i] * bflo(gwv[i]) + v[2 * i + 1] * bfhi(gwv[i]);
        }
        s1 = wave_sum(s1); gd = wave_sum(gd);
        const float mean = s1 * (1.f / 512.f);
        float s2 = 0.f;
#pragma unroll
        for (int i = 0; i < 8; ++i) { v[i] -= mean; s2 += v[i] * v[i]; }
        s2 = wave_sum(s2);
        const float rs = __builtin_amdgcn_rsqf(s2 * (1.f / 512.f) + 1e-5f);
        u32x4 hv;
#pragma unroll
        for (int i = 0; i < 4; ++i) hv[i] = pk(v[2 * i] * rs, v[2 * i + 1] * rs);
        ldswr(LDS_LN + (2 * a + r) * 1088 + lane16, hv);
        gate[r] = __builtin_amdgcn_rcpf(1.f + __builtin_amdgcn_exp2f(gd * -1.4426950408889634f));
      }
      sub_bar<63>(CW_AB, 4, lane);
      const unsigned ta = LDS_LN + q4 * 1088 + (128 * a + 16 * g4 + 4 * p4) * 2;
      const u32x4 h0 = cat(trrd(ta), trrd(ta + 4 * 1088));
      const u32x4 h1 = cat(trrd(ta + 128), trrd(ta + 128 + 4 * 1088));
      const unsigned hv = g4 * 65536 + l15 * 16, hs = WS_H + 8 * a * 65536 + b * 256;
      st128_sc1(h0, sWS, hv, hs);
      st128_sc1(h1, sWS, hv, hs + 4 * 65536);
      if (lane < 2) st32_sc1(__builtin_bit_cast(unsigned, lane ? gate[1] : gate[0]), sWS, lane * 4, WS_G + row0 * 4);
      bar(); bar();
      asm volatile("s_waitcnt vmcnt(0)\n\ts_barrier" : "+v"(snap0), "+v"(own), "+v"(pt0), "+v"(pt1) :: "memory");
      if (tid == 128) st32_sc1(own + 1u, sWS, 0, WS_FH + b * FS);
    }
    unsigned pfw;
    auto w1_touch = [&]() {
      const srd_t sP1 = __builtin_amdgcn_make_buffer_rsrc((void*)w1_, 0, S_ * F_ * 2, 0x00020000);
      asm volatile("buffer_load_dword %0, %1, %2, %3 offen" : "=v"(pfw) : "v"(lane < 43 ? lane * 128 : OOB), "s"(sP1), "s"(b * 22016 + a * 5504) : "memory");
    };
    unsigned h0;
    bar(); bar();
    __builtin_amdgcn_s_sleep(6); w1_touch();
    bar();
    ld32_sc1(h0, sWS, hvo, WS_FH);
    bar(); bar();
    asm volatile("s_waitcnt vmcnt(0)" : "+v"(h0) :: "memory");
    const bool hok = __all(h0 != snap0);
    if (h0 == snap0) asm volatile("buffer_load_dword %0, %1, %2, %3 offen sc1" : "+v"(h0) : "v"(hvo), "s"(sWS), "s"(WS_FH) : "memory");
    bar();
    if (a == 1 && !hok) {
      sub_wait(CW_QD, 8);
      if (lane == 0) st32_sc1(1u, sWS, 0, WS_FQ + (hg * 32 + rt) * FS);
    }
    if (!hok) {
#pragma unroll 1
      for (unsigned spin = 0; spin < SPIN_CAP; ++spin) {
        asm volatile("s_waitcnt vmcnt(0)" : "+v"(h0) :: "memory");
        if (__all(h0 != snap0)) break;
        __builtin_amdgcn_s_sleep(2);
        if (h0 == snap0) asm volatile("buffer_load_dword %0, %1, %2, %3 offen sc1" : "+v"(h0) : "v"(hvo), "s"(sWS), "s"(WS_FH) : "memory");
      }
      asm volatile("s_waitcnt vmcnt(0)" : "+v"(h0) :: "memory");
    }
    if (cw_add(CW_HR, lane) == 3 && lane == 0) st32_sc1(own + 2u, sWS, 0, WS_FH + b * FS);
    if (a == 1 && hok) {
      sub_wait(CW_QD, 8);
      if (lane == 0) st32_sc1(1u, sWS, 0, WS_FQ + (hg * 32 + rt) * FS);
    }
    {
      const unsigned head = 4 * xcd + (j >> 3), qr = j & 7;
      if (a == 1) {
        cw_wait(CW_HR, 4);
        poll_flags(sWS, WS_FQ + (xcd * 32 + (qr ? 4 * qr - 4 : 0)) * FS, (qr ? (lane & 7) : (lane & 3)) * FS);
        if (lane == 0) asm volatile("ds_add_u32 %0, %1" :: "v"(CW_HR), "v"(8u) : "memory");
      }
      cw_wait(CW_HR, 12);
      asm volatile("s_waitcnt vmcnt(0)" : "+v"(pfw) :: "memory");
      u32x4 kv[8], qA, qB;
      const bool tv = qr || a >= 2;
      const unsigned tb = head * 65536 + (8 * qr + 4 * a) * 1024 - 8192;
      const unsigned ko = tv ? WS_K + tb : OOB, vo = tv ? WS_V + tb : OOB;
      ld128_sc1i<0>(kv[0], sWS, lane16, ko); ld128_sc1i<1024>(kv[1], sWS, lane16, ko);
      ld128_sc1i<2048>(kv[2], sWS, lane16, ko); ld128_sc1i<3072>(kv[3], sWS, lane16, ko);
      const unsigned qo = WS_Q + head * 65536 + (8 * qr + 2 * a) * 1024;
      ld128_sc1i<0>(qA, sWS, lane16, qo); ld128_sc1i<1024>(qB, sWS, lane16, qo);
      unsigned gA, gB;
      ld32_sc1(gA, sWS, (lane & 31) * 4, WS_G + (256 * qr + 64 * a) * 4);
      ld32_sc1(gB, sWS, (lane & 31) * 4, WS_G + (256 * qr + 64 * a + 32) * 4);
      ld128_sc1i<0>(kv[4], sWS, lane16, vo); ld128_sc1i<1024>(kv[5], sWS, lane16, vo);
      ld128_sc1i<2048>(kv[6], sWS, lane16, vo); ld128_sc1i<3072>(kv[7], sWS, lane16, vo);
      const unsigned r = lane & 31, h = lane >> 5;
      const int tthr = (int)r - 4 * (int)h;
      const float NEG = -1e30f;
      auto cdiag = [&](float v) { f32x16 c;
#pragma unroll
        for (int i = 0; i < 16; ++i) c[i] = ((i & 3) + 8 * (i >> 2)) <= tthr ? v : NEG;
        return c; };
      auto cfirst = [&](float v) { f32x16 c;
#pragma unroll
        for (int i = 0; i < 16; ++i) c[i] = ((i & 3) + 8 * (i >> 2)) <= tthr ? NEG : v;
        return c; };
      const unsigned kb = LDS_KV, vb = kb + 16384;
      asm volatile("s_waitcnt vmcnt(4)" : "+v"(kv[0]), "+v"(kv[1]), "+v"(kv[2]), "+v"(kv[3]), "+v"(qA), "+v"(qB), "+v"(gA), "+v"(gB) :: "memory");
      if (a != 0 || r != 0) ldswr(kb + a * 4096 + lane16, kv[0]);
#pragma unroll
      for (int i = 1; i < 4; ++i) ldswr(kb + a * 4096 + i * 1024 + lane16, kv[i]);
      sub_arrive<63>(CW_AB, lane);
      asm volatile("s_waitcnt vmcnt(0)" : "+v"(kv[4]), "+v"(kv[5]), "+v"(kv[6]), "+v"(kv[7]) :: "memory");
#pragma unroll
      for (int i = 0; i < 4; ++i) ldswr(vb + a * 4096 + i * 1024 + lane16, kv[4 + i]);
      sub_wait(CW_AB, 8);
      sub_arrive<63>(CW_AB, lane);
      SETPRIO(2);
      const unsigned kaddr = kb + a * 2048 + lane16;
      const int t0 = qr ? 2 : 8 - 2 * (int)a;
      const f32x16 zero16 = {};
      auto mx16 = [](float m, const f32x16& s) {
        float m2 = fmaxf(s[14], s[15]);
#pragma unroll
        for (int i = 0; i < 8; i += 2) m = fmaxf(fmaxf(m, s[i]), s[i + 1]);
#pragma unroll
        for (int i = 8; i < 14; i += 2) m2 = fmaxf(fmaxf(m2, s[i]), s[i + 1]);
        return fmaxf(m, m2);
      };
      float mA = NEG, mB = NEG;
      {
        u32x4 k0 = ldsrd(kaddr + 8192), k1 = ldsrd(kaddr + 9216);
        u32x4 n0 = ldsrd(kaddr + 6144), n1 = ldsrd(kaddr + 7168);
        {
          const f32x16 cd = cdiag(0.f);
          const f32x16 s0 = mfma32(k0, qA, cd), s1 = mfma32(k0, qB, zero16), s2 = mfma32(k1, qB, cd);
          mA = mx16(mA, s0); mB = mx16(mB, s1); mB = mx16(mB, s2);
        }
#pragma unroll 1
        for (int t = 6; t >= t0; t -= 2) {
          k0 = n0; k1 = n1;
          n0 = ldsrd(kaddr + t * 1024 - 2048); n1 = ldsrd(kaddr + t * 1024 - 1024);
          const f32x16 s0 = mfma32(k0, qA, zero16), s1 = mfma32(k0, qB, zero16), s2 = mfma32(k1, qA, zero16), s3 = mfma32(k1, qB, zero16);
          mA = mx16(mA, s0); mB = mx16(mB, s1); mA = mx16(mA, s2); mB = mx16(mB, s3);
        }
        if (qr) {
          const f32x16 cf = cfirst(0.f);
          const f32x16 s0 = mfma32(n0, qA, cf), s1 = mfma32(n1, qA, zero16), s2 = mfma32(n1, qB, cf);
          mA = mx16(mA, s0); mA = mx16(mA, s1); mB = mx16(mB, s2);
        }
      }
      {
        auto rr = __builtin_amdgcn_permlane32_swap(__float_as_uint(mA), __float_as_uint(mB), false, false);
        const float mm = fmaxf(__uint_as_float(rr[0]), __uint_as_float(rr[1]));
        auto r2 = __builtin_amdgcn_permlane32_swap(__float_as_uint(mm), __float_as_uint(mm), false, false);
        mA = -__uint_as_float(r2[0]); mB = -__uint_as_float(r2[1]);
      }
      f32x16 oA = {}, oB = {}, nA, nB, sA, sB;
#pragma unroll
      for (int i = 0; i < 16; ++i) { nA[i] = mA; nB[i] = mB; }
      const u32x4 ones = {0x3f803f80u, 0x3f803f80u, 0x3f803f80u, 0x3f803f80u};
      u32x4 vf[3][2] = {{ones, ones}, {ones, ones}, {ones, ones}};
      u32x4 kf[2];
      const unsigned vaddr = vb + a * 2048 + h * 256 + r * 16;
      auto ldv = [&](int t) { if (r < 16) { vf[t % 3][0] = ldsrd(vaddr + t * 1024); vf[t % 3][1] = ldsrd(vaddr + t * 1024 + 512); } };
      auto ex8 = [&](const f32x16& s, int o) { u32x4 p;
#pragma unroll
        for (int i = 0; i < 4; ++i) p[i] = pk(__builtin_amdgcn_exp2f(s[o + 2 * i]), __builtin_amdgcn_exp2f(s[o + 2 * i + 1]));
        return p; };
      kf[0] = ldsrd(kaddr + 8192); kf[1] = ldsrd(kaddr + 9216);
      sB = mfma32(kf[1], qB, cdiag(mB));
      sA = mfma32(kf[0], qA, cdiag(mA));
      kf[1] = ldsrd(kaddr + 7168);
      sub_wait(CW_AB, 12);
      ldv(9); ldv(8);
      const int lastB = qr ? 8 : 2 * (int)a + 1;
#pragma unroll
      for (int i = 0; i <= 8; ++i) {
        if (i == 8) {
#pragma unroll
          for (int e = 0; e < 16; ++e) { const bool le = ((e & 3) + 8 * (e >> 2)) <= tthr; sA[e] = le ? NEG : sA[e]; sB[e] = le ? NEG : sB[e]; }
        }
        if (i < 8) ldv(7 - i);
        SBAR();
        {
          const u32x4 p0 = ex8(sB, 0); SBAR();
          oB = mfma32(vf[(9 - i) % 3][0], p0, oB); SBAR();
          const u32x4 p1 = ex8(sB, 8); SBAR();
          oB = mfma32(vf[(9 - i) % 3][1], p1, oB); SBAR();
        }
        if ((i & 1) && i == lastB) break;
        {
          const u32x4 p0 = ex8(sA, 0); SBAR();
          if (i < 8) sB = mfma32(kf[(8 - i) & 1], qB, nB);
          if (i < 7) kf[(8 - i) & 1] = ldsrd(kaddr + (6 - i) * 1024);
          SBAR();
          const u32x4 p1 = ex8(sA, 8); SBAR();
          oA = mfma32(vf[(8 - i) % 3][0], p0, oA);
          if (i < 8) sA = mfma32(kf[(7 - i) & 1], qA, nA);
          oA = mfma32(vf[(8 - i) % 3][1], p1, oA); SBAR();
        }
      }
      const float iA = __builtin_amdgcn_rcpf(oA[8]) * __builtin_bit_cast(float, gA), iB = __builtin_amdgcn_rcpf(oB[8]) * __builtin_bit_cast(float, gB);
      u32x4 ovA, ovB;
#pragma unroll
      for (int i = 0; i < 4; ++i) { ovA[i] = pk(oA[2 * i] * iA, oA[2 * i + 1] * iA); ovB[i] = pk(oB[2 * i] * iB, oB[2 * i + 1] * iB); }
      const unsigned ovo = (r >> 4) * 16384 + h * 256 + (r & 15) * 16;
      const unsigned oso = WS_O + (16 * qr + 4 * a) * 16384 + (head >> 1) * 1024 + (head & 1) * 512;
      SETPRIO(3);
      st128_sc1(ovA, sWS, ovo, oso);
      st128_sc1(ovB, sWS, ovo, oso + 32768);
      waitv<0>();
      if (cw_add(CW_OD, lane) == 3 && lane == 0) st32_sc1(1u, sWS, 0, WS_FO + (qr * 32 + head) * FS);
    }
    if (a == 1) {
      cw_wait(CW_OD, 4);
      poll_flags(sWS, WS_FO + (st >> 2) * 32 * FS, (lane & 31) * FS);
      cw_set(CW_OR);
    }
    return;
  }

  const unsigned w = wv - 4;
  const unsigned r8 = lane >> 3, c8 = lane & 7;
  const unsigned wslot = w * 16384;
  const unsigned csw = (c8 ^ (((r8 >> 1) & 3) << 1)) << 4;
  const srd_t sW1 = __builtin_amdgcn_make_buffer_rsrc((void*)w1_, 0, S_ * F_ * 2, 0x00020000);
  const unsigned vw1 = (ft == 21 && csw >= 64) ? OOB : r8 * (F_ * 2) + csw;
  constexpr unsigned P1 = 8 * F_ * 2 - 1024;
  const unsigned w1base = m_act ? w * (256 * F_ * 2) + ft * 128 : OOB;
  auto w1_issue = [&](int i) {
    const unsigned so = w1base + (unsigned)i * (32 * F_ * 2);
    dma4((i & 3) * 4096 + wslot, sW1, vw1, vw1 + P1, vw1 + 2 * P1, so, sW1, vw1 + 3 * P1, so);
  };

  {
    const srd_t sXd = __builtin_amdgcn_make_buffer_rsrc((void*)((const char*)x_ - 3072), 0, S_ * D_ * 2 + 3072, 0x00020000);
    const srd_t sWqkv = __builtin_amdgcn_make_buffer_rsrc((void*)wqkv_, 0, D_ * 1536 * 2, 0x00020000);
    auto qkv_issue = [&](int c) {
      const unsigned kc = ((unsigned)c + rotq) & 7;
      dma4((c & 3) * 4096 + wslot, sWqkv, vw, vw, vw, kc * (64 * 3072) + hg * 128, sXd, vx, rt * 65536 + kc * 128);
    };
    const unsigned mh = w >> 2, hd = w & 3;
    f32x4 aq[2] = {}, ak[2] = {}, av[2] = {};
    const unsigned xa0 = (4 * mh + (l15 >> 3)) * 16384 + 3072 + (l15 & 7) * 128 + ((g4 ^ ((l15 >> 1) & 7)) << 4);
    const unsigned xa1 = xa0 ^ 64;
    const unsigned fr = ((q4 >> 1) & 1) | ((g4 & 1) << 1);
    const unsigned wa0 = g4 * 16384 + q4 * 128 + ((hd ^ fr) << 5) + p4 * 8;
    unsigned wa1 = wa0 + 65536; PIN(wa1);
    u32x4 fx[2][4], fw[2][6];
    auto rdx = [&](int c, int i) { fx[c & 1][i] = ldsrd(((i >> 1) ? xa1 : xa0) + (c & 3) * 4096 + (i & 1) * 32768); };
    auto rdw = [&](int c, int i) { const unsigned a_ = (i >= 3 ? wa1 : wa0) + (c & 3) * 4096 + (i % 3) * 1024; fw[c & 1][i] = cat(trrd(a_), trrd(a_ + 512)); };
    auto rd = [&](int c) {
      rdx(c, 0); rdx(c, 1); rdw(c, 0); rdw(c, 1); rdw(c, 2); rdx(c, 2); rdx(c, 3); rdw(c, 3); rdw(c, 4); rdw(c, 5); SBAR(); };
    auto m1 = [&](int c, int ks, int t, int u) {
      const int s = c & 1;
      if (t == 0) aq[u] = mfma16(fw[s][3 * ks], fx[s][2 * ks + u], aq[u]);
      else if (t == 1) ak[u] = mfma16(fw[s][3 * ks + 1], fx[s][2 * ks + u], ak[u]);
      else av[u] = mfma16(fx[s][2 * ks + u], fw[s][3 * ks + 2], av[u]);
      SBAR();
    };
    auto mm = [&](int c, int ks) { m1(c, ks, 0, 0); m1(c, ks, 0, 1); m1(c, ks, 1, 0); m1(c, ks, 1, 1); m1(c, ks, 2, 0); m1(c, ks, 2, 1); };
    auto rdmm = [&](int c) {
      rdx(c, 0); SBAR(); m1(c - 1, 0, 0, 0); rdx(c, 1); SBAR(); m1(c - 1, 0, 0, 1);
      rdw(c, 0); SBAR(); m1(c - 1, 0, 1, 0); rdw(c, 1); SBAR(); m1(c - 1, 0, 1, 1);
      rdw(c, 2); SBAR(); m1(c - 1, 0, 2, 0); rdx(c, 2); SBAR(); m1(c - 1, 0, 2, 1);
      rdx(c, 3); SBAR(); m1(c - 1, 1, 0, 0); rdw(c, 3); SBAR(); m1(c - 1, 1, 0, 1);
      rdw(c, 4); SBAR(); m1(c - 1, 1, 1, 0); rdw(c, 5); SBAR(); m1(c - 1, 1, 1, 1);
      m1(c - 1, 1, 2, 0); m1(c - 1, 1, 2, 1);
    };
    waitbar<12>(); rd(0);
    waitbar<8>(); qkv_issue(4); rdmm(1);
    waitbar<8>(); qkv_issue(5); rdmm(2);
    waitbar<8>(); qkv_issue(6); rdmm(3);
    waitbar<8>(); qkv_issue(7); rdmm(4);
    waitbar<8>(); rdmm(5);
    waitbar<4>(); rdmm(6);
    waitbar<0>(); w1_issue(0); w1_issue(1); rdmm(7);
    mm(7, 0); mm(7, 1);
    const float qs = 0.25f * 1.4426950408889634f;
    const unsigned so = (4 * hg + hd) * 65536 + (2 * rt + mh) * 1024;
    {
      auto r0 = __builtin_amdgcn_permlane16_swap(pk(ak[0][0], ak[0][1]), pk(ak[1][0], ak[1][1]), false, false);
      auto r1 = __builtin_amdgcn_permlane16_swap(pk(ak[0][2], ak[0][3]), pk(ak[1][2], ak[1][3]), false, false);
      st128_sc1(u32x4{r0[0], r1[0], r0[1], r1[1]}, sWS, lane16, WS_K + so);
    }
    {
      auto r0 = __builtin_amdgcn_permlane32_swap(pk(av[0][0], av[0][1]), pk(av[1][0], av[1][1]), false, false);
      auto r1 = __builtin_amdgcn_permlane32_swap(pk(av[0][2], av[0][3]), pk(av[1][2], av[1][3]), false, false);
      st128_sc1(u32x4{r0[0], r1[0], r0[1], r1[1]}, sWS, lane16, WS_V + so);
    }
    {
      auto r0 = __builtin_amdgcn_permlane16_swap(pk(aq[0][0] * qs, aq[0][1] * qs), pk(aq[1][0] * qs, aq[1][1] * qs), false, false);
      auto r1 = __builtin_amdgcn_permlane16_swap(pk(aq[0][2] * qs, aq[0][3] * qs), pk(aq[1][2] * qs, aq[1][3] * qs), false, false);
      st128_sc1(u32x4{r0[0], r1[0], r0[1], r1[1]}, sWS, lane16, WS_Q + so);
    }
    bar();
    sub_arrive<0>(CW_QD, lane);
    SETPRIO(1);
    cw_wait(CW_HR, 4);
  }

  const bool opt = w < 3;
  const unsigned k0 = 5 * w + (opt ? w : 3);
  const srd_t sW2 = __builtin_amdgcn_make_buffer_rsrc((void*)w2_, 0, F_ * S_ * 2, 0x00020000);
  const unsigned rotf = dd < 5 ? dd : dd - 5;
  auto ku = [&](int i) { unsigned t = (unsigned)i + rotf; t = (i < 5 && t >= 5) ? t - 5 : t; return k0 + (i < 5 ? t : 5u); };
  auto w2_issue = [&](int i) {
    unsigned v = r8 * 4096 + csw; PIN(v);
    const unsigned pf = 8 * 4096 - 1024, so = ku(i) * (32 * 4096) + st * 128;
    dma4((i & 3) * 4096 + wslot, sW2, v, v + pf, v + 2 * pf, so, sW2, v + 3 * pf, so);
  };

  unsigned tr[4];
  {
    const unsigned t0 = wslot + (4 * g4 + q4) * 128 + ((((2 * g4 + (q4 >> 1)) & 3)) << 5) + p4 * 8;
#pragma unroll
    for (int l = 0; l < 4; ++l) tr[l] = t0 ^ (unsigned)(l << 5);
  }
  {
    u32x4 hf[4][3];
    const unsigned hv2 = dt < 10 ? lane16 + 131072 : OOB;
    const unsigned hbase = m_act ? WS_H + 3 * dt * 65536 + w * 8192 : OOB;
    auto h_issue = [&](int i) {
      const unsigned so = hbase + (unsigned)i * 1024;
      ld128_sc1(hf[i & 3][0], sWS, lane16, so);
      ld128_sc1(hf[i & 3][1], sWS, lane16 + 65536, so);
      ld128_sc1(hf[i & 3][2], sWS, hv2, so);
    };
    u32x2 bb0, bb1;
    const srd_t sB1 = __builtin_amdgcn_make_buffer_rsrc((void*)b1_, 0, F_ * 2, 0x00020000);
    const unsigned fbase = 64 * ft + 32 * (w & 1);
    ld64(bb0, sB1, g4 * 8, fbase * 2);
    ld64(bb1, sB1, g4 * 8, fbase * 2 + 32);
    h_issue(0);
    f32x4 acc[4][3] = {};
#define HPIN(i) "+v"(hf[(i) & 3][0]), "+v"(hf[(i) & 3][1]), "+v"(hf[(i) & 3][2])
#define MSTEP(i, NEXT) do { SETPRIO((i) < 3 ? 1 : 0); asm volatile("s_waitcnt vmcnt(0)" : HPIN(i) :: "memory"); NEXT; SBAR(); \
      gemm_step<3, false>(acc, hf[(i) & 3], tr, ((i) & 3) * 4096); } while (0)
#define MSTEP2(i, NEXT) do { SETPRIO((i) < 3 ? 1 : 0); asm volatile("s_waitcnt vmcnt(4)" : HPIN(i) :: "memory"); NEXT; SBAR(); \
      gemm_step<3, false>(acc, hf[(i) & 3], tr, ((i) & 3) * 4096); } while (0)
    unsigned pf0, pf1;
    auto w2_touch = [&]() {
      const srd_t sPo = __builtin_amdgcn_make_buffer_rsrc((void*)wo_, 0, D_ * D_ * 2, 0x00020000);
      const unsigned v2 = (lane >> 2) * 4096 + (lane & 3) * 128, s2 = j * 43 * 4096 + xcd * 512;
      if (w == 6) {
        asm volatile("buffer_load_dword %0, %1, %2, %3 offen" : "=v"(pf0) : "v"(v2), "s"(sW2), "s"(s2) : "memory");
        asm volatile("buffer_load_dword %0, %1, %2, %3 offen" : "=v"(pf1) : "v"(v2), "s"(sW2), "s"(s2 + 65536) : "memory");
      } else if (w == 7) {
        asm volatile("buffer_load_dword %0, %1, %2, %3 offen" : "=v"(pf0) : "v"(lane < 44 ? v2 : OOB), "s"(sW2), "s"(s2 + 131072) : "memory");
        asm volatile("buffer_load_dword %0, %1, %2, %3 offen" : "=v"(pf1) : "v"(lane < 16 ? lane * 128 : OOB), "s"(sPo), "s"(b * 2048) : "memory");
      } else {
        asm volatile("" : "=v"(pf0), "=v"(pf1));
      }
    };
    MSTEP(0, (h_issue(1), w1_issue(2)));
    PIN(bb0); PIN(bb1);
    MSTEP2(1, (h_issue(2), w1_issue(3)));
    MSTEP2(2, (h_issue(3), w1_issue(4)));
    MSTEP2(3, (h_issue(4), w1_issue(5)));
    MSTEP2(4, (h_issue(5), w1_issue(6)));
    MSTEP2(5, (h_issue(6), w1_issue(7)));
    MSTEP2(6, h_issue(7));
    MSTEP(7, w2_touch());
    SETPRIO(3);
#pragma unroll
    for (int l = 0; l < 4; ++l)
#pragma unroll
      for (int r = 0; r < 3; ++r) ldswr((r + 1) * 4096 + wslot + l * 1024 + lane16, __builtin_bit_cast(u32x4, acc[l][r]));
    sub_bar<63>(CW_GB, 8, lane);
    if (w < 6) {
      const unsigned ra = ((w >> 1) + 1) * 4096 + (w & 1) * 2048 + lane16;
      unsigned rb = ra + 65536; PIN(rb);
      f32x4 t0[8], t1[8];
#pragma unroll
      for (int ww = 0; ww < 8; ++ww) {
        t0[ww] = __builtin_bit_cast(f32x4, ldsrd((ww < 4 ? ra : rb) + (ww & 3) * 16384));
        t1[ww] = __builtin_bit_cast(f32x4, ldsrd((ww < 4 ? ra : rb) + (ww & 3) * 16384 + 1024));
      }
      SBAR();
      f32x4 s0 = ((t0[0] + t0[1]) + (t0[2] + t0[3])) + ((t0[4] + t0[5]) + (t0[6] + t0[7]));
      f32x4 s1 = ((t1[0] + t1[1]) + (t1[2] + t1[3])) + ((t1[4] + t1[5]) + (t1[6] + t1[7]));
      PIN(s0); PIN(s1);
      sub_arrive<63>(CW_GB, lane);
      float u[8] = {s0[0] + bflo(bb0.x), s0[1] + bfhi(bb0.x), s0[2] + bflo(bb0.y), s0[3] + bfhi(bb0.y),
                    s1[0] + bflo(bb1.x), s1[1] + bfhi(bb1.x), s1[2] + bflo(bb1.y), s1[3] + bfhi(bb1.y)};
#pragma unroll
      for (int i = 0; i < 8; ++i) {
        const float t = u[i] * (1.f + 0.044715f * u[i] * u[i]) * (-2.f * 0.7978845608028654f * 1.4426950408889634f);
        u[i] = u[i] * __builtin_amdgcn_rcpf(1.f + __builtin_amdgcn_exp2f(t));
      }
      const u32x4 uv = {pk(u[0], u[1]), pk(u[2], u[3]), pk(u[4], u[5]), pk(u[6], u[7])};
      const unsigned du = 3 * dt + (w >> 1), ks = 2 * ft + (w & 1);
      st128_sc1(uv, sWS, lane16, (m_act && du < 32 && ks < 43) ? WS_U + (du * 43 + ks) * 1024 : OOB);
      waitv<0>();
      if (cw_add(CW_UD, lane) == 5 && lane == 0 && m_act) st32_sc1(1u, sWS, 0, WS_FU + (dt * 32 + ft) * FS);
    }
    sub_wait(CW_GB, 14);
    asm volatile("s_waitcnt vmcnt(0)" : "+v"(pf0), "+v"(pf1) :: "memory");
  }

  {
    SETPRIO(0);
    w2_issue(0); w2_issue(1); w2_issue(2); w2_issue(3);
    unsigned gs[4], gq, b2q;
    const srd_t sB2 = __builtin_amdgcn_make_buffer_rsrc((void*)b2_, 0, S_ * 2, 0x00020000);
#pragma unroll
    for (int u = 0; u < 4; ++u) ld32_sc1(gs[u], sWS, l15 * 4, WS_G + (64 * st + 16 * u) * 4);
    ld32_sc1(gq, sWS, r8 * 4, WS_G + (64 * st + 8 * w) * 4);
    ld16u(b2q, sB2, r8 * 2, (64 * st + 8 * w) * 2);
    {
      const unsigned fl = (k0 + (opt ? 5 : 4)) >> 1;
      unsigned fi = (k0 >> 1) + (lane & 3); fi = fi > fl ? fl : fi;
      poll_flags(sWS, WS_FU + ((4 * dd) / 3) * 32 * FS, (((lane >> 2) & 1) * 32 + fi) * FS);
    }
    asm volatile("" : "+v"(gs[0]), "+v"(gs[1]), "+v"(gs[2]), "+v"(gs[3]), "+v"(gq), "+v"(b2q));
    u32x4 df[3][4];
    auto u_issue = [&](int i) {
      const unsigned so = WS_U + 4 * dd * 43 * 1024 + ku(i) * 1024;
#pragma unroll
      for (int r = 0; r < 4; ++r) ld128_sc1(df[i % 3][r], sWS, lane16, so + r * (43 * 1024));
    };
    const srd_t sWo = __builtin_amdgcn_make_buffer_rsrc((void*)wo_, 0, D_ * D_ * 2, 0x00020000);
    auto wo_issue = [&](int i) {
      unsigned v = csw; PIN(v); v += r8 * 1024;
      const unsigned pf = 8 * 1024 - 1024, so = (2 * w + i - 6) * (32 * 1024) + dd * 128;
      dma4((i & 3) * 4096 + wslot, sWo, v, v + pf, v + 2 * pf, so, sWo, v + 3 * pf, so);
    };
    auto o_issue = [&]() {
      const unsigned so = WS_O + 4 * st * 16384 + 2 * w * 1024;
#pragma unroll
      for (int r = 0; r < 4; ++r) ld128_sc1i<0>(df[0][r], sWS, lane16, so + r * 16384);
#pragma unroll
      for (int r = 0; r < 4; ++r) ld128_sc1i<1024>(df[1][r], sWS, lane16, so + r * 16384);
    };
    SETPRIO(1);
    u_issue(0); u_issue(1);
    f32x4 acc[4][4] = {};
#define DPIN(i) "+v"(df[(i) % 3][0]), "+v"(df[(i) % 3][1]), "+v"(df[(i) % 3][2]), "+v"(df[(i) % 3][3])
#define FSTEP(i, N) do { asm volatile("s_waitcnt vmcnt(" #N ")" : DPIN(i) :: "memory"); gemm_step<4, true>(acc, df[(i) % 3], tr, ((i) & 3) * 4096); } while (0)
    FSTEP(0, 4); u_issue(2); w2_issue(4);
    FSTEP(1, 8); if (opt) w2_issue(5); u_issue(3);
    FSTEP(2, 8); u_issue(4); wo_issue(6);
    FSTEP(3, 8);
    u32x4 dopt[4];
    if (opt) {
      const unsigned so = WS_U + 4 * dd * 43 * 1024 + ku(5) * 1024;
#pragma unroll
      for (int r = 0; r < 4; ++r) ld128_sc1(dopt[r], sWS, lane16, so + r * (43 * 1024));
    } else {
#pragma unroll
      for (int r = 0; r < 4; ++r) asm volatile("" : "=v"(dopt[r]));
    }
    wo_issue(7);
    FSTEP(4, 8);
    if (opt) {
      asm volatile("s_waitcnt vmcnt(4)" : "+v"(dopt[0]), "+v"(dopt[1]), "+v"(dopt[2]), "+v"(dopt[3]) :: "memory");
      gemm_step<4, true>(acc, dopt, tr, 4096);
    }
    cw_wait(CW_OR, 1);
    o_issue();
#pragma unroll
    for (int u = 0; u < 4; ++u) {
      const float og = 1.f - __builtin_bit_cast(float, gs[u]);
#pragma unroll
      for (int r = 0; r < 4; ++r) acc[u][r] *= og;
    }
    {
      unsigned ly = lane; PIN(ly);
      const unsigned rowA = 16 * (ly >> 5) + 4 * ((ly >> 4) & 1) + ((ly >> 2) & 3);
      const unsigned t0 = wslot + rowA * 128 + (((rowA >> 1) & 3) << 5) + (ly & 3) * 8;
#pragma unroll
      for (int i = 6; i < 8; ++i) {
        if (i == 6) asm volatile("s_waitcnt vmcnt(4)" : DPIN(6) :: "memory"); else asm volatile("s_waitcnt vmcnt(0)" : DPIN(7) :: "memory");
        u32x4 wf[4];
#pragma unroll
        for (int l = 0; l < 4; ++l) { const unsigned ta = (t0 ^ (unsigned)(l << 5)) + (i & 3) * 4096; wf[l] = cat(trrd(ta), trrd(ta + 1024)); }
        SBAR();
#pragma unroll
        for (int l = 0; l < 4; ++l)
#pragma unroll
          for (int u = 0; u < 4; ++u) acc[u][l] = mfma16(wf[l], df[i % 3][u], acc[u][l]);
        SBAR();
      }
    }
    unsigned lz = threadIdx.x; PIN(lz); lz &= 63;
    const unsigned g4 = lz >> 4, l15 = lz & 15, r8 = lz >> 3, c8 = lz & 7;
    {
      const unsigned sa0 = wslot + l15 * 256 + ((g4 ^ (l15 & 7)) << 4), sa1 = sa0 ^ 64;
#pragma unroll
      for (int su = 0; su < 4; ++su)
#pragma unroll
        for (int du = 0; du < 4; ++du) ldswr(((du & 1) ? sa1 : sa0) + (du >> 1) * 128 + su * 4096, __builtin_bit_cast(u32x4, acc[su][du]));
    }
    bar();
    const unsigned ra0 = (8 * w + r8) * 256 + (((2 * c8) ^ r8) << 4), ra1 = ra0 ^ 16;
    unsigned rb0 = ra0 + 65536, rb1 = ra1 + 65536; PIN(rb0); PIN(rb1);
    f32x4 t0[8], t1[8];
#pragma unroll
    for (int ww = 0; ww < 8; ++ww) {
      t0[ww] = __builtin_bit_cast(f32x4, ldsrd((ww < 4 ? ra0 : rb0) + (ww & 3) * 16384));
      t1[ww] = __builtin_bit_cast(f32x4, ldsrd((ww < 4 ? ra1 : rb1) + (ww & 3) * 16384));
    }
    SBAR();
    const f32x4 y0 = ((t0[0] + t0[1]) + (t0[2] + t0[3])) + ((t0[4] + t0[5]) + (t0[6] + t0[7]));
    const f32x4 y1 = ((t1[0] + t1[1]) + (t1[2] + t1[3])) + ((t1[4] + t1[5]) + (t1[6] + t1[7]));
    const float bb = bflo(b2q) * (1.f - __builtin_bit_cast(float, gq));
    const u32x4 ov = {pk(y0[0] + bb, y0[1] + bb), pk(y0[2] + bb, y0[3] + bb), pk(y1[0] + bb, y1[1] + bb), pk(y1[2] + bb, y1[3] + bb)};
    const srd_t sOut = __builtin_amdgcn_make_buffer_rsrc(out_, 0, S_ * D_ * 2, 0x00020000);
    st128(ov, sOut, r8 * 1024 + c8 * 16, (64 * st + 8 * w) * 1024 + dd * 128);
  }
}

extern "C" void kernel_launch(void* const* d_in, const int* in_sizes, int n_in, void* d_out, int out_size, void* d_ws,
                              size_t ws_size, hipStream_t stream) {
  (void)in_sizes; (void)n_in; (void)out_size; (void)ws_size;
  (void)hipFuncSetAttribute((const void*)k_main, hipFuncAttributeMaxDynamicSharedMemorySize, LDS_BYTES);
  k_main<<<256, 768, LDS_BYTES, stream>>>(d_in[0], d_in[1], d_in[2], d_in[4], d_in[5], d_in[6], d_in[7], d_in[8], d_out, d_ws);
}
```
